```python
import torch

def _softmax(x, axis=-1):
    return torch.softmax(x, dim=axis)
def _gelu(x):
    return torch.nn.functional.gelu(x, approximate="tanh")
def _silu(x):
    return torch.nn.functional.silu(x)
def _sigmoid(x):
    return torch.sigmoid(x)
def _layernorm(x, axis=-1, eps=1e-5):
    m = x.mean(dim=axis, keepdim=True)
    v = x.var(dim=axis, keepdim=True, unbiased=False)
    return (x - m) / torch.sqrt(v + eps)
def _scatter_add(tgt, idx, src, axis=0):
    return tgt.clone().index_add_(axis, idx, src)
def _segment_sum(src, seg, n):
    out = torch.zeros((n,) + src.shape[1:], dtype=src.dtype)
    return out.index_add_(0, seg, src)
def _pad(x, pw):
    flat = tuple(v for pair in reversed(pw) for v in pair)
    return torch.nn.functional.pad(x, flat)

kernel_name = 'parallel_sum__linear_kernel_hedgehog__dense_activation_squared_relu__prompt'

def setup_inputs():

    _w = (lambda a: (lambda t: t.to(torch.bfloat16) if t.is_floating_point() else t)(torch.as_tensor(a)))
    def _A_setup():
        B, S, D, H = 1, 2048, 1024, 32
        Dh = D // H
        return {
            "x": _w((lambda *s: torch.randn(*s, dtype=torch.bfloat16) * 0.05)(B, S, D)),
            "wqkv": _w((lambda *s: torch.randn(*s, dtype=torch.bfloat16) * 0.8)(D, 3 * D)),
            "w_phi": _w((lambda *s: torch.randn(*s, dtype=torch.bfloat16) * 0.8)(H, Dh, Dh) * (Dh ** -0.5)),
            "wo": _w((lambda *s: torch.randn(*s, dtype=torch.bfloat16) * 0.2)(D, D)),
        }
    _A_in = _A_setup()

    def _B_setup():
        B, S, D, F = 1, 2048, 1024, 3584
        return {
            "x": _w((lambda *s: torch.randn(*s, dtype=torch.bfloat16) * 0.05)(B, S, D)),
            "w1": _w((lambda *s: torch.randn(*s, dtype=torch.bfloat16) * 0.05)(D, F)),
            "b1": _w((lambda *s: torch.randn(*s, dtype=torch.bfloat16) * 1.6)(F)),
            "w2": _w((lambda *s: torch.randn(*s, dtype=torch.bfloat16) * 0.05)(F, D)),
            "b2": _w((lambda *s: torch.randn(*s, dtype=torch.bfloat16) * 12.8)(D)),
        }
    _B_in = _B_setup()

    return {
        "A_x": _A_in["x"],
        "A_wqkv": _A_in["wqkv"],
        "A_w_phi": _A_in["w_phi"],
        "A_wo": _A_in["wo"],
        "B_x": _B_in["x"],
        "B_w1": _B_in["w1"],
        "B_b1": _B_in["b1"],
        "B_w2": _B_in["w2"],
        "B_b2": _B_in["b2"],
    }

def reference(A_x, A_wqkv, A_w_phi, A_wo, B_x, B_w1, B_b1, B_w2, B_b2):
    def _A_ref(x, wqkv, w_phi, wo):
        B, S, D = x.shape
        H = 32; Dh = D // H
        qkv = (lambda x, *d: x.permute(*d))((x @ wqkv).reshape(B, S, 3, H, Dh), 2, 0, 3, 1, 4)
        q, k, v = qkv[0], qkv[1], qkv[2]
        pq = torch.einsum("bhsd,hdf->bhsf", q, w_phi)
        pk = torch.einsum("bhsd,hdf->bhsf", k, w_phi)
        phi_q = torch.concatenate([_softmax(pq, axis=-1), _softmax(-pq, axis=-1)], axis=-1)
        phi_k = torch.concatenate([_softmax(pk, axis=-1), _softmax(-pk, axis=-1)], axis=-1)
        kv = torch.einsum("bhsf,bhsd->bhfd", phi_k, v)
        z  = torch.einsum("bhsf->bhf", phi_k)
        num = torch.einsum("bhqf,bhfd->bhqd", phi_q, kv)
        den = torch.einsum("bhqf,bhf->bhq", phi_q, z)[..., None] + 1e-6
        o = num / den
        return (lambda x, *d: x.permute(*d))(o, 0, 2, 1, 3).reshape(B, S, D) @ wo

    def _B_ref(x, w1, b1, w2, b2):
        h = torch.einsum("bsd,df->bsf", x, w1) + b1
        zero = torch.zeros_like(h)
        r = torch.where(h > zero, h, zero)
        h = r * r
        out = torch.einsum("bsf,fd->bsd", h, w2) + b2
        return out

    x = A_x
    xn = _layernorm(x, axis=-1)
    return x + _A_ref(xn, A_wqkv, A_w_phi, A_wo) + _B_ref(xn, B_w1, B_b1, B_w2, B_b2)
```

```cpp
#include <hip/hip_runtime.h>
#include <hip/hip_bf16.h>
#define ROUND_XN 1
#define ROUND_PQ 1
typedef unsigned short u16;
__device__ __forceinline__ float b2f(u16 v) { return __uint_as_float(((unsigned)v) << 16); }
__device__ __forceinline__ u16 f2b(float f) { unsigned u = __float_as_uint(f); u += 0x7fffu + ((u >> 16) & 1u); return (u16)(u >> 16); }
__device__ __forceinline__ float rb(float f) { return b2f(f2b(f)); }
constexpr int S = 2048, D = 1024, F = 3584, H = 32;

__global__ void ln_kernel(const u16* x, float* xn) {
  int row = blockIdx.x; __shared__ float red[256];
  float v[4]; float s = 0;
  for (int i = 0; i < 4; ++i) { v[i] = b2f(x[row * D + threadIdx.x * 4 + i]); s += v[i]; }
  red[threadIdx.x] = s; __syncthreads();
  for (int o = 128; o > 0; o >>= 1) { if (threadIdx.x < o) red[threadIdx.x] += red[threadIdx.x + o]; __syncthreads(); }
  float m = red[0] / D; __syncthreads();
  s = 0; for (int i = 0; i < 4; ++i) s += (v[i] - m) * (v[i] - m);
  red[threadIdx.x] = s; __syncthreads();
  for (int o = 128; o > 0; o >>= 1) { if (threadIdx.x < o) red[threadIdx.x] += red[threadIdx.x + o]; __syncthreads(); }
  float var = red[0] / D;
  float mb = rb(m), sb = rb(sqrtf(rb(rb(var) + 1e-5f)));
  for (int i = 0; i < 4; ++i) {
    xn[row * D + threadIdx.x * 4 + i] = rb(rb(v[i] - mb) / sb);
  }
}
__global__ void gemm(const float* A, int lda, const u16* B, int ldb, float* C, int ldc, int K, int accum) {
  __shared__ float As[64][33], Bs[32][65];
  int tx = threadIdx.x & 15, ty = threadIdx.x >> 4, bm = blockIdx.y * 64, bn = blockIdx.x * 64;
  float acc[4][4] = {};
  for (int k0 = 0; k0 < K; k0 += 32) {
    for (int i = threadIdx.x; i < 64 * 32; i += 256) { As[i >> 5][i & 31] = A[(size_t)(bm + (i >> 5)) * lda + k0 + (i & 31)]; Bs[i >> 6][i & 63] = b2f(B[(size_t)(k0 + (i >> 6)) * ldb + bn + (i & 63)]); }
    __syncthreads();
    for (int k = 0; k < 32; ++k) { float a[4], b[4]; for (int i = 0; i < 4; ++i) { a[i] = As[ty * 4 + i][k]; b[i] = Bs[k][tx * 4 + i]; }
      for (int i = 0; i < 4; ++i) for (int j = 0; j < 4; ++j) acc[i][j] += a[i] * b[j]; }
    __syncthreads();
  }
  for (int i = 0; i < 4; ++i) for (int j = 0; j < 4; ++j) { float* p = &C[(size_t)(bm + ty * 4 + i) * ldc + bn + tx * 4 + j]; *p = accum ? *p + acc[i][j] : acc[i][j]; }
}
__global__ void feat(const float* qkv, const u16* wphi, float* phi) {
  int idx = blockIdx.x * blockDim.x + threadIdx.x;
  int h = idx & 31, s = (idx >> 5) & 2047, w = idx >> 16;
  float p[32];
#pragma unroll
  for (int f = 0; f < 32; ++f) p[f] = 0;
  for (int d = 0; d < 32; ++d) { float q = rb(qkv[(size_t)s * 3072 + w * 1024 + h * 32 + d]);
#pragma unroll
    for (int f = 0; f < 32; ++f) p[f] += q * b2f(wphi[(h * 32 + d) * 32 + f]); }
  float mx = -1e30f, mn = 1e30f;
#pragma unroll
  for (int f = 0; f < 32; ++f) {
    p[f] = rb(p[f]);
    mx = fmaxf(mx, p[f]); mn = fminf(mn, p[f]); }
  float s1 = 0, s2 = 0;
#pragma unroll
  for (int f = 0; f < 32; ++f) { s1 += __expf(p[f] - mx); s2 += __expf(mn - p[f]); }
  float* o = phi + (((size_t)w * S + s) * H + h) * 64;
#pragma unroll
  for (int f = 0; f < 32; ++f) { o[f] = rb(__expf(p[f] - mx) / s1); o[32 + f] = rb(__expf(mn - p[f]) / s2); }
}
__global__ void kvk(const float* qkv, const float* phi, float* kv) {
  int h = blockIdx.x, f = blockIdx.y, d = threadIdx.x & 63, part = threadIdx.x >> 6; __shared__ float red[4][64];
  float a = 0;
  if (d < 33) for (int s = part; s < S; s += 4) { float ph = phi[(((size_t)S + s) * H + h) * 64 + f]; float v = d < 32 ? rb(qkv[(size_t)s * 3072 + 2048 + h * 32 + d]) : 1.f; a += ph * v; }
  red[part][d] = a; __syncthreads();
  if (part == 0 && d < 33) kv[(h * 64 + f) * 33 + d] = rb(red[0][d] + red[1][d] + red[2][d] + red[3][d]);
}
__global__ void ok(const float* phi, const float* kv, float* o) {
  int idx = blockIdx.x * blockDim.x + threadIdx.x; int d = idx & 31, h = (idx >> 5) & 31, s = idx >> 10;
  float n = 0, de = 0;
  for (int f = 0; f < 64; ++f) { float ph = phi[((size_t)s * H + h) * 64 + f]; n += ph * kv[(h * 64 + f) * 33 + d]; de += ph * kv[(h * 64 + f) * 33 + 32]; }
  o[(size_t)s * 4608 + h * 32 + d] = rb(n / (de + 1e-6f));
}
__global__ void act(float* hb, const u16* b1) {
  int idx = blockIdx.x * blockDim.x + threadIdx.x; int f = idx % F, s = idx / F;
  float* p = &hb[(size_t)s * 4608 + 1024 + f]; float v = fmaxf(*p + b2f(b1[f]), 0.f); *p = rb(v * v);
}
__global__ void fin(const float* c, const u16* x, const u16* b2, u16* out) {
  int idx = blockIdx.x * blockDim.x + threadIdx.x; out[idx] = f2b(c[idx] + b2f(x[idx]) + b2f(b2[idx & 1023]));
}
extern "C" void kernel_launch(void* const* d_in, const int* in_sizes, int n_in, void* d_out, int out_size, void* d_ws, size_t ws_size, hipStream_t stream) {
  const u16 *x = (const u16*)d_in[0], *wqkv = (const u16*)d_in[1], *wphi = (const u16*)d_in[2], *wo = (const u16*)d_in[3];
  const u16 *w1 = (const u16*)d_in[5], *b1 = (const u16*)d_in[6], *w2 = (const u16*)d_in[7], *b2 = (const u16*)d_in[8];
  float* ws = (float*)d_ws;
  float* xn = ws; float* qkv = xn + S * D; float* oh = qkv + S * 3072; float* phi = oh + (size_t)S * 4608; float* kv = phi + (size_t)2 * S * H * 64; float* c = kv + H * 64 * 33;
  ln_kernel<<<S, 256, 0, stream>>>(x, xn);
  gemm<<<dim3(3072 / 64, S / 64), 256, 0, stream>>>(xn, D, wqkv, 3072, qkv, 3072, D, 0);
  gemm<<<dim3(F / 64, S / 64), 256, 0, stream>>>(xn, D, w1, F, oh + 1024, 4608, D, 0);
  feat<<<2 * S * H / 256, 256, 0, stream>>>(qkv, wphi, phi);
  kvk<<<dim3(H, 64), 256, 0, stream>>>(qkv, phi, kv);
  ok<<<S * D / 256, 256, 0, stream>>>(phi, kv, oh);
  act<<<S * F / 256, 256, 0, stream>>>(oh, b1);
  gemm<<<dim3(D / 64, S / 64), 256, 0, stream>>>(oh, 4608, wo, D, c, D, D, 0);
  gemm<<<dim3(D / 64, S / 64), 256, 0, stream>>>(oh + 1024, 4608, w2, D, c, D, F, 1);
  fin<<<S * D / 256, 256, 0, stream>>>(c, x, b2, (u16*)d_out);
}
```

```cpp
#include <hip/hip_runtime.h>
#include <hip/hip_bf16.h>
#include <type_traits>

#define LAS __attribute__((address_space(3)))
#define GAS __attribute__((address_space(1)))
#define DI __device__ __forceinline__
typedef unsigned short u16;
typedef short s16x4 __attribute__((ext_vector_type(4)));
typedef __bf16 bf16x8 __attribute__((ext_vector_type(8)));
typedef __bf16 bf16x2 __attribute__((ext_vector_type(2)));
typedef float f32x2 __attribute__((ext_vector_type(2)));
typedef float f32x4 __attribute__((ext_vector_type(4)));
typedef unsigned u32x2 __attribute__((ext_vector_type(2)));
typedef unsigned u32x4 __attribute__((ext_vector_type(4)));
typedef int i32x4 __attribute__((ext_vector_type(4)));
typedef GAS unsigned gu32;

constexpr int S_ = 2048, D_ = 1024, F_ = 3584, KO = F_ + D_;
constexpr int XL = D_;
constexpr unsigned KSLAB = 128 * 1024;
constexpr size_t WS_FLAG = 0;
constexpr size_t WS_STAT = 65536;
constexpr size_t WS_CS = WS_STAT + 16384;
constexpr size_t WS_OH = 1 << 20;
constexpr int KVP_B = 9216;
constexpr size_t WS_KVP = WS_OH + (size_t)S_ * KO * 2;
constexpr size_t WS_P2 = WS_KVP + (size_t)16 * 8 * KVP_B;
constexpr int FL_KV = 0;
constexpr int FL_P2 = 4096;
constexpr int FL_RDY = 12288;
constexpr int FL_EPOCH = 16352;
constexpr unsigned SPIN_CAP = 1u << 20;
constexpr int CODE_LINES = 10;
constexpr unsigned OOB = 0x40000000u;

#define RLX_AGENT __ATOMIC_RELAXED, __HIP_MEMORY_SCOPE_AGENT
#define BAR() __builtin_amdgcn_s_barrier()
#define SCHED() __builtin_amdgcn_sched_barrier(0)
#define WAIT_V(n) asm volatile("s_waitcnt vmcnt(%0)" ::"n"(n) : "memory")
#define WAIT_L0() do { asm volatile("" ::: "memory"); __builtin_amdgcn_s_waitcnt(0xc07f); asm volatile("" ::: "memory"); } while (0)
#define MFMA16(a, b, c) __builtin_amdgcn_mfma_f32_16x16x32_bf16((a), (b), (c), 0, 0, 0)

DI float b2f(unsigned v) { return __uint_as_float(v << 16); }
DI unsigned pk2(float lo, float hi) { f32x2 v = {lo, hi}; return __builtin_bit_cast(unsigned, __builtin_convertvector(v, bf16x2)); }
DI float rbf(float x) { return __uint_as_float(pk2(x, 0.f) << 16); }
DI i32x4 make_srd(const void* p, unsigned bytes) {
  const unsigned long long a = (unsigned long long)p;
  i32x4 r; r[0] = (int)(unsigned)a; r[1] = (int)(unsigned)(a >> 32); r[2] = (int)bytes; r[3] = 0x00020000; return r;
}
DI void dma16(unsigned lds, unsigned voff, i32x4 srd, unsigned soff) {
  asm volatile("s_mov_b32 m0, %0\n\ts_nop 0\n\tbuffer_load_dwordx4 %1, %2, %3 offen lds" ::"s"(lds), "v"(voff), "s"(srd), "s"(soff) : "memory");
}
DI void store16_dev(u32x4 d, __amdgpu_buffer_rsrc_t rs, unsigned voff, unsigned soff) {
  __builtin_amdgcn_raw_buffer_store_b128(d, rs, voff, soff, 16);
  asm volatile("s_nop 1" ::"v"(d));
}
DI bf16x8 lds_b128(unsigned addr) { return *(const LAS bf16x8*)(size_t)addr; }
DI f32x4 lds_f4(unsigned addr) { return *(const LAS f32x4*)(size_t)addr; }
DI s16x4 lds_tr(unsigned addr) { return __builtin_amdgcn_ds_read_tr16_b64_v4i16((LAS s16x4*)(size_t)addr); }
DI bf16x8 lds_tr2(unsigned a0, unsigned a1) { s16x4 x = lds_tr(a0), y = lds_tr(a1); return __builtin_bit_cast(bf16x8, __builtin_shufflevector(x, y, 0, 1, 2, 3, 4, 5, 6, 7)); }
DI bf16x8 pack8(f32x4 a, f32x4 b) { u32x4 r = {pk2(a[0], a[1]), pk2(a[2], a[3]), pk2(b[0], b[1]), pk2(b[2], b[3])}; return __builtin_bit_cast(bf16x8, r); }
DI float max3(float a, float b, float c) { return __builtin_fmaxf(__builtin_fmaxf(a, b), c); }
DI float max2(float a, float b) { float ninf = -__builtin_inff(); asm("" : "+s"(ninf)); return max3(a, b, ninf); }
template <bool SUM> DI void red2(float& x, float& y) {
  auto a = __builtin_amdgcn_permlane16_swap(__float_as_uint(x), __float_as_uint(y), false, false);
  float m = SUM ? __uint_as_float(a[0]) + __uint_as_float(a[1]) : max2(__uint_as_float(a[0]), __uint_as_float(a[1]));
  auto b = __builtin_amdgcn_permlane32_swap(__float_as_uint(m), __float_as_uint(m), false, false);
  m = SUM ? __uint_as_float(b[0]) + __uint_as_float(b[1]) : max2(__uint_as_float(b[0]), __uint_as_float(b[1]));
  auto c = __builtin_amdgcn_permlane16_swap(__float_as_uint(m), __float_as_uint(m), false, false);
  x = __uint_as_float(c[0]); y = __uint_as_float(c[1]);
}
DI void pf_line(unsigned lds, unsigned voff, i32x4 srd, unsigned soff) {
  asm volatile("s_mov_b32 m0, %0\n\ts_nop 0\n\tbuffer_load_dword %1, %2, %3 offen lds" ::"s"(lds), "v"(voff), "s"(srd), "s"(soff) : "memory");
}

DI float dpp_add(float v, int ctrl_sel) {
  const int x = __builtin_bit_cast(int, v);
  const int y = ctrl_sel == 0 ? __builtin_amdgcn_update_dpp(0, x, 0xB1, 0xf, 0xf, true)
              : ctrl_sel == 1 ? __builtin_amdgcn_update_dpp(0, x, 0x4E, 0xf, 0xf, true)
              : ctrl_sel == 2 ? __builtin_amdgcn_update_dpp(0, x, 0x141, 0xf, 0xf, true)
                              : __builtin_amdgcn_update_dpp(0, x, 0x140, 0xf, 0xf, true);
  return v + __builtin_bit_cast(float, y);
}
typedef GAS unsigned long long gu64;
DI unsigned long long granule(unsigned tag, unsigned val) { return ((unsigned long long)tag << 32) | val; }

constexpr int G1_A = 0;
constexpr int G1_B = 65536;
constexpr int G1_X = 151552;
constexpr int G1_R = G1_X + 4096, G1_M = G1_R + 1024, G1_CS = G1_M + 1024, G1_BV = G1_CS + 1024, G1_TAG = G1_BV + 1024;
constexpr int G1_LDS = 163840;

struct Args { const u16* w2; const u16* wo; const u16* wqkv; const u16* wphi; const u16* w1; const u16* b1; u16* oh; char* kvp; unsigned* flags;
              const u16* x; const u16* b2; u16* out; char* p2; };

constexpr int G2_RING = 4, G2_TILE = 32768;
constexpr int G2_E = 2;
constexpr int G2_D = 2;
constexpr int PFX = 4;
DI void dma4_dev(unsigned lds, unsigned voff, i32x4 srd, unsigned soff) {
  asm volatile("s_mov_b32 m0, %0\n\ts_nop 0\n\tbuffer_load_dwordx4 %1, %2, %3 offen sc1 lds\n\tbuffer_load_dwordx4 %1, %2, %3 offen offset:1024 sc1 lds\n\t"
               "buffer_load_dwordx4 %1, %2, %3 offen offset:2048 sc1 lds\n\tbuffer_load_dwordx4 %1, %2, %3 offen offset:3072 sc1 lds" ::"s"(lds), "v"(voff), "s"(srd), "s"(soff) : "memory");
}
DI void dma4v(unsigned lds, unsigned v0, unsigned v1, unsigned v2, unsigned v3, i32x4 srd, unsigned soff) {
  asm volatile("s_mov_b32 m0, %0\n\ts_nop 0\n\tbuffer_load_dwordx4 %1, %5, %6 offen lds\n\tbuffer_load_dwordx4 %2, %5, %6 offen offset:1024 lds\n\t"
               "buffer_load_dwordx4 %3, %5, %6 offen offset:2048 lds\n\tbuffer_load_dwordx4 %4, %5, %6 offen offset:3072 lds" ::"s"(lds), "v"(v0), "v"(v1), "v"(v2), "v"(v3), "s"(srd), "s"(soff) : "memory");
}
DI void wait_flags8(gu32* fl, unsigned tag, int lane) {
  _Pragma("unroll 1") for (unsigned spins = 0; spins < SPIN_CAP; ++spins) {
    const unsigned v = __hip_atomic_load(fl + (lane & 7), RLX_AGENT);
    if (__all(v == tag)) break;
    __builtin_amdgcn_s_sleep(1);
  }
  asm volatile("" ::: "memory");
}

struct G2Tile { int kh, rt, ctile, nt, rot; };
struct G2Load {
  unsigned vB[4], vA, cA, cB;
  DI G2Load(const G2Tile& T, int lane, int wq) {
    const int i16 = lane & 15, g4 = lane >> 4;
#pragma unroll
    for (int i = 0; i < 4; ++i) { const int j = (i16 >> 1) ^ (g4 | ((i & 1) << 2)); vB[i] = (4 * i + g4) * 2048 - i * 1024 + (j & 3) * 32 + (j >> 2) * 512 + (lane & 1) * 16; }
    vA = lane * 16; cA = (wq >> 1) * KSLAB + (T.rt * 8 + 4 * (wq & 1)) * 1024; cB = wq * 16 * 2048 + (T.ctile >> 2) * 1024 + (T.ctile & 3) * 128;
  }
};

DI void featmax(const f32x4 (&p)[4][2], const float (&ls)[4], float (&mx)[4], float (&mn)[4]) {
#pragma unroll
  for (int m = 0; m < 4; ++m) {
    mx[m] = __builtin_fmaxf(max3(p[m][0][3], p[m][1][0], p[m][1][1]), max3(p[m][1][2], p[m][1][3], max3(p[m][0][0], p[m][0][1], p[m][0][2])));
    mn[m] = __builtin_fmaxf(max3(-p[m][0][3], -p[m][1][0], -p[m][1][1]), max3(-p[m][1][2], -p[m][1][3], max3(-p[m][0][0], -p[m][0][1], -p[m][0][2])));
  }
#pragma unroll
  for (int m = 0; m < 4; ++m) { red2<false>(mx[m], mn[m]); mx[m] *= -ls[m]; mn[m] *= -ls[m]; }
}
DI void featexp(const f32x4 (&p)[2], float L, float mx, float mn, f32x2 (&e)[4], f32x2 (&f)[4]) {
  const f32x2 a = {mx, mx}, b = {mn, mn}, lp = {L, L}, ln = {-L, -L};
#pragma unroll
  for (int i = 0; i < 4; ++i) {
    const f32x2 v = {p[i >> 1][(i & 1) * 2], p[i >> 1][(i & 1) * 2 + 1]};
    const f32x2 x = __builtin_elementwise_fma(v, lp, a), y = __builtin_elementwise_fma(v, ln, b);
    e[i] = (f32x2){__builtin_amdgcn_exp2f(x[0]), __builtin_amdgcn_exp2f(x[1])};
    f[i] = (f32x2){__builtin_amdgcn_exp2f(y[0]), __builtin_amdgcn_exp2f(y[1])};
  }
}
DI unsigned pk2v(f32x2 v) { return __builtin_bit_cast(unsigned, __builtin_convertvector(v, bf16x2)); }

DI int ffn_col(int wc, int n) { return n < 6 ? 6 * wc + n : 12 + wc; }

template <int TYPE>
DI unsigned gemm1_body(const Args& g, int panel, int ct, int rot, const G2Tile& T) {
  constexpr int NT = TYPE ? 6 : 7, BROW = TYPE ? 384 : 448, BBUF = 64 * BROW, NB = TYPE ? 3 : 4, LDW = (TYPE ? 3 * D_ : F_) * 2;
  const int tid = threadIdx.x, w = __builtin_amdgcn_readfirstlane(tid >> 6);
  const int grp = w >> 2, wq = w & 3, wr = (w >> 1) & 1, wc = w & 1;
  int lane = tid & 63, i16 = lane & 15, g4 = lane >> 4, q4 = i16 >> 2, p4 = lane & 3;
  const i32x4 srdA = make_srd(g.x, S_ * XL * 2), srdB = make_srd(TYPE ? g.wqkv : g.w1, D_ * LDW);
  constexpr int PB = TYPE ? 1024 : 896;
  unsigned vA[4], vB[4];
#pragma unroll
  for (int i = 0; i < 4; ++i) vA[i] = (8 * i + (lane >> 3)) * (XL * 2) - 1024 * i + (((lane & 7) ^ ((4 * (i & 1) + (lane >> 4)) & 7)) << 4);
  const unsigned soffA0 = (unsigned)(panel * 256 + (1 - grp) * 128 + 32 * wq) * (XL * 2u), ldsA0 = G1_A + (1 - grp) * 32768 + wq * 4096;
#pragma unroll
  for (int i = 0; i < NB; ++i) {
    if (TYPE == 0) { const int c = lane % 28, pos = (c >> 1) ^ (w & 1), j = ffn_col(pos >= 7, pos >= 7 ? pos - 7 : pos); vB[i] = (2 * i + lane / 28) * LDW - PB * i + (j * 2 + (c & 1)) * 16; }
    else { const int idx = 64 * i + lane, row = idx / 24, c = idx % 24, f = ((row >> 1) & 1) | ((w & 1) << 1), j = (c >> 1) ^ f;
      vB[i] = row * LDW - PB * i + (j >> 2) * 2048 + (j & 3) * 32 + (c & 1) * 16; }
  }
  const unsigned soffB0 = (unsigned)(8 * w) * LDW + ct * (TYPE ? 128 : 448), ldsB0 = G1_B + 8 * w * BROW;
#define SET_M0(x) asm volatile("s_mov_b32 m0, %0" ::"s"(x) : "memory")
#define PIECE_A(i, so) asm volatile("buffer_load_dwordx4 %0, %1, %2 offen offset:%3 lds" ::"v"(vA[i]), "s"(srdA), "s"(so), "n"(1024 * (i)) : "memory")
#define PIECE_B(i, so) do { if (TYPE == 0) asm volatile("s_mov_b32 exec_hi, 0xffffff\n\tbuffer_load_dwordx4 %0, %1, %2 offen offset:%3 lds\n\ts_mov_b32 exec_hi, -1" ::"v"(vB[i]), "s"(srdB), "s"(so), "n"(PB * (i)) : "memory"); \
    else asm volatile("buffer_load_dwordx4 %0, %1, %2 offen offset:%3 lds" ::"v"(vB[i]), "s"(srdB), "s"(so), "n"(PB * (i)) : "memory"); } while (0)
#define TILE_A(t) do { const unsigned so_ = soffA0 + ((t) ^ rot) * 128; SET_M0(ldsA0 + ((t) & 1) * 16384); asm volatile("s_nop 0"); PIECE_A(0, so_); PIECE_A(1, so_); PIECE_A(2, so_); PIECE_A(3, so_); } while (0)
#define TILE_B(t) do { const unsigned so_ = soffB0 + (unsigned)(((t) ^ rot) * 64) * LDW; SET_M0(ldsB0 + (t) * BBUF); asm volatile("s_nop 0"); PIECE_B(0, so_); PIECE_B(1, so_); PIECE_B(2, so_); if (NB > 3) PIECE_B(NB - 1, so_); } while (0)
  unsigned rA[2], rB[2];
#pragma unroll
  for (int s = 0; s < 2; ++s) rA[s] = G1_A + grp * 32768 + (wr * 64 + i16) * 128 + (((4 * s + g4) ^ (i16 >> 1)) << 4);
#pragma unroll
  for (int e = 0; e < 2; ++e) {
    if (TYPE == 0) rB[e] = G1_B + (8 * g4 + q4) * BROW + 7 * wc * 32 + p4 * 8 + ((g4 & 1) ? (((wc + e) & 1) ? -32 : 32) : 0);
    else rB[e] = G1_B + (8 * g4 + q4) * BROW + (((2 * wc + e) ^ (((q4 >> 1) & 1) | ((g4 & 1) << 1))) << 5) + p4 * 8;
  }
  TILE_B(0);
  if (TYPE == 0) { if (w == 0) { const i32x4 sb = make_srd(g.b1 + ct * 224, 448); dma16(G1_X, lane * 16, sb, 0); } }
  else { if (w < 4) { const i32x4 sp = make_srd(g.wphi + ct * 2048, 4096); dma16(G1_X + w * 1024, lane * 16, sp, w * 1024); } }
  const int srow = panel * 256 + (TYPE * 16 + ct) * 8 + 2 * wq;
  u32x4 xr[4]; unsigned ep;
  if (grp == 1) asm volatile("global_load_dword %0, %1, off sc1" : "=v"(ep) : "v"(g.flags + FL_EPOCH) : "memory");
  if (grp == 1) asm volatile("buffer_load_dwordx4 %0, %4, %5, %6 offen\n\tbuffer_load_dwordx4 %1, %4, %5, %6 offen offset:1024\n\t"
                             "buffer_load_dwordx4 %2, %4, %5, %6 offen offset:2048\n\tbuffer_load_dwordx4 %3, %4, %5, %6 offen offset:3072"
                             : "=&v"(xr[0]), "=&v"(xr[1]), "=&v"(xr[2]), "=&v"(xr[3]) : "v"(lane * 16), "s"(srdA), "s"(srow * 2048) : "memory");
  TILE_B(1);
  if (grp == 1) TILE_A(1);
  f32x4 acc[4][NT];
#pragma unroll
  for (int m = 0; m < 4; ++m)
#pragma unroll
    for (int n = 0; n < NT; ++n) { acc[m][n] = (f32x4){0.f, 0.f, 0.f, 0.f}; asm volatile("" : "+v"(acc[m][n])); }
  asm volatile("" : "+v"(rA[0]), "+v"(rA[1]), "+v"(rB[0]), "+v"(rB[1]));
  if (grp == 1) WAIT_V(9 + NB); else WAIT_V(NB);
  BAR();
  unsigned tag = 0;
  if (grp == 1) {
    asm volatile("s_waitcnt vmcnt(%5)" : "+v"(xr[0]), "+v"(xr[1]), "+v"(xr[2]), "+v"(xr[3]), "+v"(ep) : "n"(4 + NB) : "memory");
    tag = __builtin_amdgcn_readfirstlane(ep) + 1u;
    float sum[2] = {0.f, 0.f}, sq[2] = {0.f, 0.f};
#pragma unroll
    for (int i = 0; i < 8; ++i)
#pragma unroll
      for (int r = 0; r < 2; ++r) { const unsigned u = xr[2 * r + (i >> 2)][i & 3]; const bf16x2 v = __builtin_bit_cast(bf16x2, u);
        sum[r] = __builtin_amdgcn_fdot2_f32_bf16(v, __builtin_bit_cast(bf16x2, 0x3f803f80u), sum[r], false); sq[r] = __builtin_amdgcn_fdot2_f32_bf16(v, v, sq[r], false); }
#pragma unroll
    for (int k = 0; k < 4; ++k)
#pragma unroll
      for (int r = 0; r < 2; ++r) { sum[r] = dpp_add(sum[r], k); sq[r] = dpp_add(sq[r], k); }
#pragma unroll
    for (int r = 0; r < 2; ++r) { red2<true>(sum[r], sq[r]); sum[r] *= 1.f / D_; sq[r] = fmaxf(sq[r] * (1.f / D_) - sum[r] * sum[r], 0.f); }
    const unsigned ms = pk2(lane ? sum[1] : sum[0], __builtin_amdgcn_sqrtf(rbf(rbf(lane ? sq[1] : sq[0]) + 1e-5f)));
    if (lane < 2) __hip_atomic_store((gu64*)((char*)g.flags + WS_STAT) + srow + lane, granule(tag, ms), RLX_AGENT);
    *(LAS unsigned*)(size_t)G1_TAG = tag;
    BAR();
  }
  gu64* csg = (gu64*)((char*)g.flags + WS_CS) + (TYPE * 16 + ct) * (8 * 224);
  const int cs_t = (w >> 1) == 2 ? (panel & 3) : -1;
  float cs0 = 0.f, cs1 = 0.f;
  u32x2 gs, gc[8];
  unsigned a0 = rA[0], a1 = rA[1], b0 = rB[0], b1 = rB[1], ringR = 0, ringW = 2 * BBUF;
  unsigned sa = soffA0 + ((1 + grp) ^ rot) * 128, la = ldsA0 + ((1 + grp) & 1) * 16384, sb = soffB0 + (unsigned)((2 ^ rot) * 64) * LDW, lb = ldsB0 + ringW;
  auto body = [&](auto modec, int t) {
    constexpr int MODE = decltype(modec)::value;
    bf16x8 Af[4][2], Bf[NT][2];
    if (MODE != 0) asm volatile("" : "+v"(a0), "+v"(a1), "+v"(b0), "+v"(b1));
    auto rdB = [&](int n, int s) { const unsigned base = (n & 1) ? b1 : b0; const int co = TYPE ? (n >> 1) * 128 : n * 32;
      Bf[n][s] = lds_tr2(base + co + (32 * s) * BROW, base + co + (32 * s + 4) * BROW); };
    if (MODE == 1 && grp == 0) { SET_M0(la); asm volatile("s_nop 0"); PIECE_A(0, sa); PIECE_A(1, sa); PIECE_A(2, sa); PIECE_A(3, sa); }
    if (MODE == 0) SET_M0(la);
    Af[0][0] = lds_b128(a0); Af[1][0] = lds_b128(a0 + 2048);
    if (MODE == 0) PIECE_A(0, sa);
    Af[2][0] = lds_b128(a0 + 4096); Af[3][0] = lds_b128(a0 + 6144); rdB(0, 0);
    if (MODE == 0) PIECE_A(1, sa);
    rdB(1, 0); rdB(2, 0);
    if (MODE == 0) PIECE_A(2, sa);
    rdB(3, 0); rdB(4, 0);
    if (MODE == 0) { PIECE_A(3, sa); SET_M0(lb); }
    rdB(5, 0); if (NT > 6) rdB(NT - 1, 0);
    if (MODE == 0) PIECE_B(0, sb);
    Af[0][1] = lds_b128(a1); Af[1][1] = lds_b128(a1 + 2048); Af[2][1] = lds_b128(a1 + 4096); Af[3][1] = lds_b128(a1 + 6144);
    if (MODE == 0) PIECE_B(1, sb);
    rdB(0, 1); rdB(1, 1);
    if (MODE == 0) PIECE_B(2, sb);
    rdB(2, 1); rdB(3, 1);
    if (MODE == 0 && NB > 3) PIECE_B(NB - 1, sb);
    rdB(4, 1); rdB(5, 1); if (NT > 6) rdB(NT - 1, 1);
    if (MODE != 0 && grp == 0) {
      const auto rg = __builtin_amdgcn_make_buffer_rsrc((void*)((char*)g.flags + WS_STAT), 0, (unsigned)(WS_OH - WS_STAT), 0x00020000);
      const unsigned sc = (unsigned)(WS_CS - WS_STAT) + (TYPE * 16 + ct) * (8 * 224 * 8);
      auto fetch = [&]() {
        gs = __builtin_amdgcn_raw_buffer_load_b64(rg, tid * 8, panel * 2048, 16);
#pragma unroll
        for (int pp = 0; pp < 8; ++pp) gc[pp] = __builtin_amdgcn_raw_buffer_load_b64(rg, tid * 8, sc + pp * 1792, 16);
      };
      if (MODE == 1) { WAIT_V(4); fetch(); }
      else {
        _Pragma("unroll 1") for (unsigned spins = 0; spins < SPIN_CAP; ++spins) {
          bool ok = true;
#pragma unroll
          for (int pp = 0; pp < 8; ++pp) ok &= gc[pp][1] == tag;
          if (__all(gs[1] == tag && (ok || tid >= NT * 32))) break;
          __builtin_amdgcn_s_sleep(1);
          asm volatile("" ::: "memory");
          fetch();
        }
        float c = 0.f;
#pragma unroll
        for (int pp = 0; pp < 8; ++pp) c += __uint_as_float(gc[pp][0]);
        if (TYPE == 0) { const int pos = tid >> 4; *(LAS float*)(size_t)(G1_BV + tid * 4) = 0.5f * b2f(*(const LAS u16*)(size_t)(G1_X + (ffn_col(pos >= 7, pos >= 7 ? pos - 7 : pos) * 16 + i16) * 2)); }
        *(LAS float*)(size_t)(G1_R + tid * 4) = (TYPE ? 1.f : 0.5f) * __builtin_amdgcn_rcpf(__uint_as_float(gs[0] & 0xffff0000u));
        *(LAS float*)(size_t)(G1_M + tid * 4) = -b2f(gs[0] & 0xffffu);
        *(LAS float*)(size_t)(G1_CS + tid * 4) = c;
      }
    }
    int docs = 0;
    if (MODE != 2) {
      const unsigned dR = ringR == 2 * BBUF ? -2 * BBUF : BBUF;
      ringR += dR; ringW = ringW == 2 * BBUF ? 0 : ringW + BBUF;
      a0 ^= 16384; a1 ^= 16384; b0 += dR; b1 += dR;
      sa = soffA0 + ((t + 2 + grp) ^ rot) * 128; la ^= 16384; sb = soffB0 + (unsigned)(((t + 3) ^ rot) * 64) * LDW; lb = ldsB0 + ringW;
      docs = (t >> 1) ^ cs_t;
      asm volatile("" ::"s"(sa), "s"(la), "s"(sb), "s"(lb), "s"(ringR), "s"(ringW), "v"(a0), "v"(a1), "v"(b0), "v"(b1));
      asm volatile("" : "+s"(docs));
    }
    if (MODE == 0) WAIT_V(4 + NB); else if (MODE == 2 || grp == 1) WAIT_V(0);
    WAIT_L0();
    SCHED();
    BAR();
    SCHED();
#pragma unroll
    for (int j = 0; j < 8 * NT; ++j) {
      const int p = j >> 1, m = p / NT, n = (m & 1) ? NT - 1 - p % NT : p % NT, s = (j ^ p) & 1;
      acc[m][n] = (TYPE == 1 && n >= 4) ? MFMA16(Af[m][s], Bf[n][s], acc[m][n]) : MFMA16(Bf[n][s], Af[m][s], acc[m][n]);
      SCHED();
    }
    if (MODE == 0 && docs == 0) {
      const u32x4 ones_u = {0x3f803f80u, 0x3f803f80u, 0x3f803f80u, 0x3f803f80u};
      const bf16x8 ones = __builtin_bit_cast(bf16x8, ones_u);
      f32x4 c[8];
#pragma unroll
      for (int n = 0; n < NT; ++n) c[n] = MFMA16(ones, Bf[n][1], MFMA16(ones, Bf[n][0], ((f32x4){0.f, 0.f, 0.f, 0.f})));
      SCHED(); BAR(); SCHED();
      const bool lo = g4 < 2, ev = !(g4 & 1);
      cs0 += lo ? (ev ? c[0][0] : c[1][0]) : (ev ? c[2][0] : c[3][0]);
      cs1 += lo ? (ev ? c[4][0] : c[5][0]) : c[NT - 1][0];
      if (t & 1) {
        gu64* dst = csg + panel * 224 + NT * 16 * wc + lane;
        __hip_atomic_store(dst, granule(tag, __float_as_uint(cs0)), RLX_AGENT);
        if (lane < (NT - 4) * 16) __hip_atomic_store(dst + 64, granule(tag, __float_as_uint(cs1)), RLX_AGENT);
      }
    } else if (MODE != 2 || grp == 0) BAR();
    SCHED();
  };
#pragma unroll 1
  for (int t = 0; t < 14; ++t) { body(std::integral_constant<int, 0>{}, t); }
  tag = __builtin_amdgcn_readfirstlane(*(const LAS unsigned*)(size_t)G1_TAG);
  body(std::integral_constant<int, 1>{}, 14);
  body(std::integral_constant<int, 2>{}, 15);
  asm volatile("" ::: "memory");
  { int tz = threadIdx.x; asm volatile("" : "+v"(tz));
    lane = tz & 63; i16 = lane & 15; g4 = lane >> 4; q4 = i16 >> 2; p4 = lane & 3; }
  const int row0 = panel * 256 + grp * 128 + wr * 64;
  unsigned tr = G1_R + ((w >> 1) * 64 + i16) * 4; asm volatile("" : "+v"(tr));
  float rs[4], nm[4];
#pragma unroll
  for (int m = 0; m < 4; ++m) { rs[m] = *(const LAS float*)(size_t)(tr + m * 64); nm[m] = *(const LAS float*)(size_t)(tr + 1024 + m * 64); }
  if constexpr (TYPE == 0) {
    const auto roh = __builtin_amdgcn_make_buffer_rsrc((void*)g.oh, 0, S_ * KO * 2, 0x00020000);
    const unsigned so = (unsigned)((ct >> 3) * 72 + (ct & 7) * 7) * KSLAB + row0 * 64;
    f32x4 cv[NT], bv[NT];
    unsigned tb = G1_CS + (NT * wc * 16 + 4 * g4) * 4; asm volatile("" : "+v"(tb));
#pragma unroll
    for (int n = NT - 1; n >= 0; --n) { cv[n] = lds_f4(tb + n * 64); bv[n] = lds_f4(tb + 1024 + n * 64); }
    auto act = [&](int m, int n) { f32x4 v = (acc[m][n] + cv[n] * nm[m]) * rs[m] + bv[n];
      float v0, v1, v2, v3;
      asm("v_add_f32 %0, %4, |%4|\n\tv_add_f32 %1, %5, |%5|\n\tv_add_f32 %2, %6, |%6|\n\tv_add_f32 %3, %7, |%7|\n\t"
          "v_mul_f32 %0, %0, %0\n\tv_mul_f32 %1, %1, %1\n\tv_mul_f32 %2, %2, %2\n\tv_mul_f32 %3, %3, %3"
          : "=&v"(v0), "=&v"(v1), "=&v"(v2), "=&v"(v3) : "v"(v[0]), "v"(v[1]), "v"(v[2]), "v"(v[3]));
      v = (f32x4){v0, v1, v2, v3};
      return (u32x2){pk2(v[0], v[1]), pk2(v[2], v[3])}; };
#pragma unroll
    for (int m = 0; m < 4; ++m) {
#pragma unroll
      for (int n = 0; n < 6; n += 2) { const u32x2 a = act(m, n), b = act(m, n + 1);
        store16_dev((u32x4){a[0], a[1], b[0], b[1]}, roh, lane * 16 + m * 1024, so + (3 * wc + (n >> 1)) * KSLAB); SCHED(); }
      if (m == 3) {
#pragma unroll
        for (int k = 0; k < 4; ++k) *(LAS u32x2*)(size_t)(131072 + ((w >> 1) * 4 + k) * 1024 + lane * 16 + wc * 8) = act(k, 6);
        WAIT_L0(); BAR(); asm volatile("" ::: "memory");
        const u32x4 d0 = *(const LAS u32x4*)(size_t)(131072 + w * 2048 + lane * 16), d1 = *(const LAS u32x4*)(size_t)(131072 + w * 2048 + 1024 + lane * 16);
        store16_dev(d0, roh, lane * 16 + 2 * wc * 1024, so + 6 * KSLAB); store16_dev(d1, roh, lane * 16 + (2 * wc + 1) * 1024, so + 6 * KSLAB);
      }
    }
  } else {
    constexpr int PHI = 0, PROW = 144, VFR = 2 * 256 * PROW, KVL = 131072;
    {
      const int xcd = blockIdx.x & 7, kh = xcd >> 2, qb = (panel & 3) * 4 + (ct & 3);
      const unsigned vo = (lane >> 3) * 2048 + (lane & 7) * 128 + (xcd & 1) * 1024;
#pragma unroll
      for (int rep = 0; rep < 2; ++rep)
        if (rep == 0 || w < PFX) {
          const int k = qb * (8 + PFX) + rep * 8 + w, nf = 32 * (PFX - 1), id = k < nf ? k : nf + (k - nf) * 2 + ((xcd >> 1) & 1);
          pf_line(G1_X + 11264, vo, id < 224 ? make_srd(g.w2, F_ * D_ * 2) : make_srd(g.wo, D_ * D_ * 2), (unsigned)(id < 224 ? 1792 * kh + 8 * id : 512 * kh + 8 * (id - 224)) * 2048u);
        }
    }
    const int qd = w >> 1;
    __builtin_amdgcn_s_setprio(2);
    bf16x8 Wp[2];
#pragma unroll
    for (int ft = 0; ft < 2; ++ft) { const unsigned a = G1_X + wc * 2048 + (4 * g4 + q4) * 64 + ft * 32 + p4 * 8; Wp[ft] = lds_tr2(a, a + 16 * 64); }
    const f32x4 z4 = {0.f, 0.f, 0.f, 0.f};
    f32x4 cv[4];
#pragma unroll
    for (int n = 0; n < 4; ++n) cv[n] = lds_f4(G1_CS + ((NT * wc + n) * 16 + 4 * g4) * 4);
    float ls[4];
#pragma unroll
    for (int m = 0; m < 4; ++m) ls[m] = rs[m] * 1.4426950408889634f;
    {
#pragma unroll
      for (int m = 0; m < 4; ++m) {
        const unsigned o = (qd * 64 + m * 16 + 4 * g4) * 4; const f32x4 r4 = lds_f4(G1_R + o), m4 = lds_f4(G1_M + o);
#pragma unroll
        for (int dt = 0; dt < 2; ++dt) acc[m][4 + dt] = (acc[m][4 + dt] + m4 * *(const LAS float*)(size_t)(G1_CS + ((NT * wc + 4 + dt) * 16 + i16) * 4)) * r4;
      }
      f32x4 p[4][2]; float mx[4], mn[4];
#pragma unroll
      for (int m = 0; m < 4; ++m) { const bf16x8 kf = pack8(acc[m][2] + cv[2] * nm[m], acc[m][3] + cv[3] * nm[m]); p[m][0] = MFMA16(Wp[0], kf, z4); p[m][1] = MFMA16(Wp[1], kf, z4); }
#pragma unroll
      for (int u = 0; u < 2; ++u)
#pragma unroll
        for (int dt = 0; dt < 2; ++dt) *(LAS bf16x8*)(size_t)(VFR + wc * 16384 + ((qd * 2 + u) * 2 + dt) * 1024 + lane * 16) = pack8(acc[2 * u][4 + dt], acc[2 * u + 1][4 + dt]);
      featmax(p, ls, mx, mn);
#pragma unroll
      for (int h = 0; h < 2; ++h) {
        f32x2 e[2][4], f[2][4]; float se[2], sf[2];
#pragma unroll
        for (int j = 0; j < 2; ++j) { featexp(p[2 * h + j], ls[2 * h + j], mx[2 * h + j], mn[2 * h + j], e[j], f[j]);
          const f32x2 a = (e[j][0] + e[j][1]) + (e[j][2] + e[j][3]), b = (f[j][0] + f[j][1]) + (f[j][2] + f[j][3]); se[j] = a[0] + a[1]; sf[j] = b[0] + b[1]; }
#pragma unroll
        for (int j = 0; j < 2; ++j) red2<true>(se[j], sf[j]);
#pragma unroll
        for (int j = 0; j < 2; ++j) {
          const float re = __builtin_amdgcn_rcpf(se[j]), rf = __builtin_amdgcn_rcpf(sf[j]);
          const unsigned a = PHI + (wc * 256 + qd * 64 + (2 * h + j) * 16 + i16) * PROW + g4 * 8;
          *(LAS u32x2*)(size_t)(a) = (u32x2){pk2v(e[j][0] * re), pk2v(e[j][1] * re)}; *(LAS u32x2*)(size_t)(a + 32) = (u32x2){pk2v(e[j][2] * re), pk2v(e[j][3] * re)};
          *(LAS u32x2*)(size_t)(a + 64) = (u32x2){pk2v(f[j][0] * rf), pk2v(f[j][1] * rf)}; *(LAS u32x2*)(size_t)(a + 96) = (u32x2){pk2v(f[j][2] * rf), pk2v(f[j][3] * rf)};
        }
      }
    }
    WAIT_L0(); BAR(); asm volatile("" ::: "memory");
    const u32x4 ones_u = {0x3f803f80u, 0x3f803f80u, 0x3f803f80u, 0x3f803f80u};
    const bf16x8 ones = __builtin_bit_cast(bf16x8, ones_u);
    f32x4 k0 = z4, k1 = z4, kz = z4;
#pragma unroll
    for (int u = 0; u < 8; ++u) {
      const unsigned a = PHI + (wc * 256 + 32 * u + 4 * g4 + q4) * PROW + qd * 32 + p4 * 8;
      const bf16x8 pf = lds_tr2(a, a + 16 * PROW), v0 = lds_b128(VFR + wc * 16384 + u * 2048 + lane * 16), v1 = lds_b128(VFR + wc * 16384 + u * 2048 + 1024 + lane * 16);
      k0 = MFMA16(pf, v0, k0); k1 = MFMA16(pf, v1, k1); kz = MFMA16(pf, ones, kz);
    }
    const auto rkv = __builtin_amdgcn_make_buffer_rsrc((void*)(g.kvp + (size_t)ct * 8 * KVP_B), 0, 8 * KVP_B, 0x00020000);
    const unsigned kvo = w * 1024 + lane * 16, zo = 8192 + w * 128 + g4 * 16;
    store16_dev(__builtin_bit_cast(u32x4, pack8(k0, k1)), rkv, kvo, panel * KVP_B);
    if (i16 < 2) store16_dev(__builtin_bit_cast(u32x4, kz), rkv, zo + i16 * 64, panel * KVP_B);
    gu32* fl = (gu32*)g.flags + FL_KV + (ct * 8 + w) * 32;
    u32x4 qp[4], qn[4];
    {
      f32x4 p[4][2]; float mx[4], mn[4];
#pragma unroll
      for (int m = 0; m < 4; ++m) { const bf16x8 qf = pack8(acc[m][0] + cv[0] * nm[m], acc[m][1] + cv[1] * nm[m]); p[m][0] = MFMA16(Wp[0], qf, z4); p[m][1] = MFMA16(Wp[1], qf, z4); }
      featmax(p, ls, mx, mn);
#pragma unroll
      for (int m = 0; m < 4; ++m) asm volatile("" : "+v"(mx[m]), "+v"(mn[m]));
      SCHED(); WAIT_V(0);
      if (lane == 0) __hip_atomic_store(fl + panel, tag, RLX_AGENT);
      __builtin_amdgcn_s_setprio(0);
      SCHED();
#pragma unroll
      for (int m = 0; m < 4; ++m) {
        f32x2 e[4], f[4];
        featexp(p[m], ls[m], mx[m], mn[m], e, f);
#pragma unroll
        for (int i = 0; i < 4; ++i) { qp[m][i] = pk2v(e[i]); qn[m][i] = pk2v(f[i]); }
        asm volatile("" : "+v"(qp[m]), "+v"(qn[m]));
      }
    }
    _Pragma("unroll 1") for (unsigned spins = 0; spins < SPIN_CAP; ++spins) {
      const unsigned v = __hip_atomic_load(fl + (lane & 7), RLX_AGENT);
      if (__all(v == tag)) break;
      __builtin_amdgcn_s_sleep(1);
    }
    asm volatile("" ::: "memory");
    {
      u32x4 ld[8];
#pragma unroll
      for (int pp = 0; pp < 8; ++pp) ld[pp] = __builtin_amdgcn_raw_buffer_load_b128(rkv, kvo, pp * KVP_B, 16);
      f32x4 zz = __builtin_bit_cast(f32x4, __builtin_amdgcn_raw_buffer_load_b128(rkv, zo + (i16 & 7) * KVP_B, 0, 16));
      f32x2 t[4] = {{0.f, 0.f}, {0.f, 0.f}, {0.f, 0.f}, {0.f, 0.f}};
#pragma unroll
      for (int pp = 0; pp < 8; ++pp)
#pragma unroll
        for (int i = 0; i < 4; ++i) { const unsigned x = ld[pp][i]; t[i] += (f32x2){__uint_as_float(x << 16), __uint_as_float(x & 0xffff0000u)}; }
#pragma unroll
      for (int r = 0; r < 4; ++r) {
        float v = zz[r];
        v += __builtin_bit_cast(float, __builtin_amdgcn_update_dpp(0, __builtin_bit_cast(int, v), 0xB1, 0xf, 0xf, true));
        v += __builtin_bit_cast(float, __builtin_amdgcn_update_dpp(0, __builtin_bit_cast(int, v), 0x4E, 0xf, 0xf, true));
        v += __builtin_bit_cast(float, __builtin_amdgcn_update_dpp(0, __builtin_bit_cast(int, v), 0x141, 0xf, 0xf, true));
        zz[r] = ((i16 & 3) == 1) ? 1.0f : v;
      }
      const unsigned kvl = KVL + wc * 6144 + (qd >> 1) * 1024 + lane * 16 + (qd & 1) * 8;
      *(LAS u32x2*)(size_t)(kvl) = (u32x2){pk2v(t[0]), pk2v(t[1])};
      *(LAS u32x2*)(size_t)(kvl + 2048) = (u32x2){pk2v(t[2]), pk2v(t[3])};
      *(LAS u32x2*)(size_t)(kvl + 4096) = (u32x2){pk2(zz[0], zz[1]), pk2(zz[2], zz[3])};
    }
    if (w == 0) wait_flags8((gu32*)g.flags + FL_RDY + ((T.rt >> 1) * 4 + T.kh) * 32, tag, lane);
    WAIT_L0(); BAR(); asm volatile("" ::: "memory");
    const G2Load L(T, lane, wq);
    auto early = [&](int t) {
      if (grp == 0) {
        dma4_dev(t * G2_TILE + wq * 4096, L.vA, make_srd(g.oh, S_ * KO * 2), L.cA + (T.kh * 72 + 2 * (t ^ T.rot)) * KSLAB);
        dma4v(t * G2_TILE + 16384 + wq * 4096, L.vB[0], L.vB[1], L.vB[2], L.vB[3], make_srd(g.w2, F_ * D_ * 2), L.cB + T.kh * 1792 * 2048 + (t ^ T.rot) * 131072);
      }
    };
    early(0);
    const auto roh = __builtin_amdgcn_make_buffer_rsrc((void*)g.oh, 0, S_ * KO * 2, 0x00020000);
    const unsigned so = (unsigned)((ct >> 3) * 72 + 56 + (ct & 7) * 2 + wc) * KSLAB + row0 * 64;
    bf16x8 ka[3][2];
#pragma unroll
    for (int c = 0; c < 3; ++c)
#pragma unroll
      for (int sg = 0; sg < 2; ++sg) ka[c][sg] = lds_b128(KVL + wc * 6144 + (c * 2 + sg) * 1024 + lane * 16);
#pragma unroll
    for (int m = 0; m < 4; ++m) {
      const bf16x8 fp = __builtin_bit_cast(bf16x8, qp[m]), fn = __builtin_bit_cast(bf16x8, qn[m]);
      const f32x4 dp = MFMA16(ka[2][0], fp, z4), dn = MFMA16(ka[2][1], fn, z4);
      const f32x4 a0 = MFMA16(ka[0][0], fp, z4), b0 = MFMA16(ka[0][1], fn, z4), a1 = MFMA16(ka[1][0], fp, z4), b1 = MFMA16(ka[1][1], fn, z4);
      const float rp = __builtin_amdgcn_rcpf(dp[1]), rn = __builtin_amdgcn_rcpf(dn[1]);
      const float rd = __builtin_amdgcn_rcpf(dp[0] * rp + dn[0] * rn + 1e-6f), cp = rp * rd, cn = rn * rd;
      const f32x4 o0 = a0 * cp + b0 * cn, o1 = a1 * cp + b1 * cn;
      store16_dev(__builtin_bit_cast(u32x4, pack8(o0, o1)), roh, lane * 16 + m * 1024, so);
      if (m == 1) early(1);
    }
  }
  return tag;
}

constexpr int G2_ADD = G2_RING * G2_TILE;
DI void dma2_dev(unsigned lds, unsigned voff, i32x4 srd, unsigned soff) {
  asm volatile("s_mov_b32 m0, %0\n\ts_nop 0\n\tbuffer_load_dwordx4 %1, %2, %3 offen sc1 lds\n\tbuffer_load_dwordx4 %1, %2, %3 offen offset:1024 sc1 lds" ::"s"(lds), "v"(voff), "s"(srd), "s"(soff) : "memory");
}

DI void gemm2_body(const Args& g, const G2Tile& T, int type, gu32* myflag, unsigned tag) {
  const int kh = T.kh, rt = T.rt, ctile = T.ctile, nt = T.nt;
  int tid = threadIdx.x; asm volatile("" : "+v"(tid));
  const int lane = tid & 63, w = __builtin_amdgcn_readfirstlane(tid >> 6);
  const int grp = w >> 2, wq = w & 3, wr = (w >> 1) & 1, wc = w & 1;
  const int i16 = lane & 15, g4 = lane >> 4, q4 = i16 >> 2, p4 = lane & 3;
  const i32x4 srdA = make_srd(g.oh, S_ * KO * 2);
  const int orow = rt * 128 + wr * 64 + kh * 32, ocol = (ctile >> 2) * 512 + (ctile & 3) * 64 + wc * 256;
  if (type == 0) { WAIT_V(0); BAR(); if (tid == 256) __hip_atomic_store(myflag, tag, RLX_AGENT); }
  if (grp == 0) {
    const G2Load L(T, lane, wq);
    const unsigned cA = L.cA, cB = L.cB;
    const i32x4 srdW2 = make_srd(g.w2, F_ * D_ * 2), srdWo = make_srd(g.wo, D_ * D_ * 2);
    unsigned sa = cA + kh * 72 * KSLAB, sb = cB + kh * 1792 * 2048;
    if (type == 0) {
#pragma unroll
      for (int t = 0; t < 4; ++t) dma4v(t * G2_TILE + 16384 + wq * 4096, L.vB[0], L.vB[1], L.vB[2], L.vB[3], srdW2, sb + (t ^ T.rot) * 131072);
      BAR();
#pragma unroll
      for (int t = 0; t < 4; ++t) dma4_dev(t * G2_TILE + wq * 4096, L.vA, srdA, sa + (t ^ T.rot) * 2 * KSLAB);
      WAIT_V(12); BAR();
      WAIT_V(8); BAR();
    } else {
      WAIT_V(8); BAR();
#pragma unroll
      for (int t = 2; t < 4; ++t) {
        dma4_dev(t * G2_TILE + wq * 4096, L.vA, srdA, sa + (t ^ T.rot) * 2 * KSLAB);
        dma4v(t * G2_TILE + 16384 + wq * 4096, L.vB[0], L.vB[1], L.vB[2], L.vB[3], srdW2, sb + (t ^ T.rot) * 131072);
        if (t == 2) { WAIT_V(8); BAR(); }
      }
    }
    const unsigned iA[2] = {(1 - 2 * T.rot) * 2 * KSLAB, (1 + 2 * T.rot) * 2 * KSLAB}, iB[2] = {(unsigned)(1 + 2 * T.rot) * 131072, (unsigned)(1 - 2 * T.rot) * 131072};
    sa += (8 + 2 * T.rot) * KSLAB; sb += (3 - T.rot) * 131072;
    static_assert(G2_E == 2 && G2_D == 2, "the loader schedule below is written out for these");
    const unsigned m0A = wq * 4096, m0H = m0A + kh * 2048;
#define G2_ONE(SL, SRDW, NW) asm volatile("s_add_u32 m0, %2, %10\n\ts_add_u32 %1, %1, %14\n\t" \
      "buffer_load_dwordx4 %3, %8, %0 offen sc1 lds\n\tbuffer_load_dwordx4 %3, %8, %0 offen offset:1024 sc1 lds\n\t" \
      "buffer_load_dwordx4 %3, %8, %0 offen offset:2048 sc1 lds\n\tbuffer_load_dwordx4 %3, %8, %0 offen offset:3072 sc1 lds\n\t" \
      "s_add_u32 m0, %2, %11\n\ts_add_u32 %0, %0, %13\n\t" \
      "buffer_load_dwordx4 %4, %9, %1 offen lds\n\tbuffer_load_dwordx4 %5, %9, %1 offen offset:1024 lds\n\t" \
      "buffer_load_dwordx4 %6, %9, %1 offen offset:2048 lds\n\tbuffer_load_dwordx4 %7, %9, %1 offen offset:3072 lds\n\t" \
      "s_waitcnt vmcnt(%12)\n\ts_barrier" \
      : "+s"(sa), "+s"(sb) : "s"(m0A), "v"(L.vA), "v"(L.vB[0]), "v"(L.vB[1]), "v"(L.vB[2]), "v"(L.vB[3]), "s"(srdA), "s"(SRDW), "n"((SL) * G2_TILE), "n"((SL) * G2_TILE + 16384), "n"(NW), "s"(iA[(SL) & 1]), "s"(iB[(SL) & 1]) : "memory", "scc")
#define G2_HALF(SL) do { dma2_dev(m0H + (SL) * G2_TILE, L.vA, srdA, sa + kh * 2048); sb += iB[(SL) & 1]; \
      dma4v(m0A + (SL) * G2_TILE + 16384, L.vB[0], L.vB[1], L.vB[2], L.vB[3], srdWo, sb); sa += iA[(SL) & 1]; WAIT_V(12); BAR(); } while (0)
    G2_ONE(0, srdW2, 12); G2_ONE(1, srdW2, 16); G2_ONE(2, srdW2, 16); G2_ONE(3, srdW2, 16);
    if (type && tid == 0) __hip_atomic_store(myflag, tag, RLX_AGENT);
#pragma unroll 1
    for (int i = 0; i < 4; ++i) { G2_ONE(0, srdW2, 16); G2_ONE(1, srdW2, 16); G2_ONE(2, srdW2, 16); G2_ONE(3, srdW2, 16); }
    gu32* rdo = (gu32*)g.flags + FL_RDY + ((rt >> 1) * 4 + 2) * 32;
    unsigned fv;
    asm volatile("global_load_dword %0, %1, off sc1" : "=v"(fv) : "v"(rdo + (lane & 7) + (lane & 8) * 4) : "memory");
    G2_ONE(0, srdW2, 16); G2_ONE(1, srdW2, 16); G2_ONE(2, srdW2, 16); G2_ONE(3, srdW2, 16);
    asm volatile("" : "+v"(fv));
    if (!__all(fv == tag)) { wait_flags8(rdo, tag, lane); wait_flags8(rdo + 32, tag, lane); }
    if (blockIdx.x == 0 && tid == 0) __hip_atomic_store((gu32*)g.flags + FL_EPOCH, tag, RLX_AGENT);
    auto seek = [&](int hf, int u) { sa = cA + (hf * 72 + 56 + 2 * (u + T.rot)) * KSLAB; sb = cB + (hf * 512 + 64 * (u - 1 - T.rot)) * 2048; };
    seek(kh, 0);
    G2_ONE(0, srdWo, 16); G2_ONE(1, srdWo, 16); G2_ONE(2, srdWo, 16); G2_ONE(3, srdWo, 16);
    if (!type) {
      G2_ONE(0, srdWo, 16); G2_ONE(1, srdWo, 16);
      seek(1 - kh, 4); G2_ONE(2, srdWo, 16); G2_ONE(3, srdWo, 16);
    }
    seek(kh, 6); G2_HALF(0); G2_HALF(1);
    u32x4 xw[4];
    const u32x4 bw = *(const u32x4*)(g.b2 + ocol + (lane & 7) * 8);
#pragma unroll
    for (int i = 0; i < 4; ++i) xw[i] = *(const u32x4*)(g.x + (size_t)(orow + i * 8 + (lane >> 3)) * D_ + ocol + (lane & 7) * 8);
    seek(1 - kh, 6); G2_HALF(2); G2_HALF(3);
    gu32* fl = (gu32*)g.flags + FL_P2 + (rt * 8 + ctile) * 32 + (1 - kh) * 4 + wq;
    WAIT_V(6); BAR();
    f32x2 fin[4][4];
#pragma unroll
    for (int i = 0; i < 4; ++i)
#pragma unroll
      for (int c = 0; c < 4; ++c) fin[i][c] = (f32x2){b2f(xw[i][c] & 0xffffu) + b2f(bw[c] & 0xffffu), __uint_as_float(xw[i][c] & 0xffff0000u) + __uint_as_float(bw[c] & 0xffff0000u)};
#pragma unroll
    for (int i = 0; i < 4; ++i) asm volatile("" : "+v"(fin[i][0]), "+v"(fin[i][1]), "+v"(fin[i][2]), "+v"(fin[i][3]));
    unsigned sr[2][2];
#pragma unroll
    for (int i = 0; i < 2; ++i)
#pragma unroll
      for (int h = 0; h < 2; ++h) { const int r = i * 8 + (lane >> 3); sr[i][h] = wq * 8192 + r * 256 + ((((lane & 7) * 2 + h) ^ r) << 4); asm volatile("" : "+v"(sr[i][h])); }
    unsigned oo = ((orow + (lane >> 3)) * D_ + ocol + (lane & 7) * 8) * 2;
    asm volatile("" : "+v"(oo));
    bool got = false;
#pragma unroll 1
    for (int k = 1; k < 5; ++k) {
      unsigned v = tag;
      if (k < 4) {
        if (k == 1) WAIT_V(0);
        if (!got) asm volatile("global_load_dword %0, %1, off sc1" : "=v"(v) : "v"(fl) : "memory");
        BAR();
        if (!got) asm volatile("s_waitcnt vmcnt(0)" : "+v"(v)::"memory");
      } else if (!got) { _Pragma("unroll 1") for (unsigned spins = 0; spins < SPIN_CAP; ++spins) { if (__all(__hip_atomic_load(fl, RLX_AGENT) == tag)) break; __builtin_amdgcn_s_sleep(1); } }
      if (!got && __all(v == tag)) {
        got = true;
        dma4_dev(G2_ADD + wq * 4096, lane * 16, make_srd(g.p2 + (size_t)(rt * 8 + ctile) * 32768, 32768), (1 - kh) * 16384 + wq * 4096);
      }
    }
    WAIT_V(0);
    {
      const int c8 = lane & 7;
      const unsigned pa = G2_ADD + wq * 4096 + (c8 >> 2) * 1024 + ((lane >> 3) + 32 * (c8 & 1)) * 16 + ((c8 >> 1) & 1) * 8;
      u32x2 pv[4][2];
#pragma unroll
      for (int i = 0; i < 4; ++i)
#pragma unroll
        for (int h = 0; h < 2; ++h) pv[i][h] = *(const LAS u32x2*)(size_t)(pa + (i >> 1) * 2048 + (i & 1) * 128 + h * 256);
#pragma unroll
      for (int i = 0; i < 4; ++i)
#pragma unroll
        for (int c = 0; c < 4; ++c) { const unsigned u = pv[i][c >> 1][c & 1]; fin[i][c] += (f32x2){__uint_as_float(u << 16), __uint_as_float(u & 0xffff0000u)}; asm volatile("" : "+v"(fin[i][c])); }
    }
    BAR(); asm volatile("" ::: "memory");
    f32x4 v[4][2];
#pragma unroll
    for (int i = 0; i < 4; ++i)
#pragma unroll
      for (int h = 0; h < 2; ++h) v[i][h] = lds_f4(sr[i & 1][h] + (i >> 1) * 4096);
#pragma unroll
    for (int i = 0; i < 4; ++i) {
      u32x4 o;
#pragma unroll
      for (int c = 0; c < 4; ++c) o[c] = pk2v(fin[i][c] + (f32x2){v[i][c >> 1][(c & 1) * 2], v[i][c >> 1][(c & 1) * 2 + 1]});
      __builtin_amdgcn_raw_buffer_store_b128(o, __builtin_amdgcn_make_buffer_rsrc((void*)g.out, 0, S_ * D_ * 2, 0x00020000), oo + i * 8 * D_ * 2, 0, 16);
    }
    return;
  }
  unsigned rA[2][2], rB[2][4];
#pragma unroll
  for (int q = 0; q < 2; ++q) {
#pragma unroll
    for (int h = 0; h < 2; ++h) {
      rA[q][h] = q * 65536 + (wr * 4 + 2 * (h ^ kh)) * 1024 + lane * 16;
      asm volatile("" : "+v"(rA[q][h])); }
#pragma unroll
    for (int n = 0; n < 4; ++n) {
      rB[q][n] = q * 65536 + 16384 + p4 * 8 + (4 * g4 + q4) * 256 + (((4 * wc + n) ^ (q4 | ((g4 & 1) << 2))) << 5);
      asm volatile("" : "+v"(rB[q][n])); }
  }
  f32x4 acc[4][4];
#pragma unroll
  for (int m = 0; m < 4; ++m)
#pragma unroll
    for (int n = 0; n < 4; ++n) { acc[m][n] = (f32x4){0.f, 0.f, 0.f, 0.f}; asm volatile("" : "+v"(acc[m][n])); }
  bf16x8 A0[4][2], A1[4][2]; s16x4 B0[4][2][2], B1[4][2][2];
  auto rd = [&](int j, int slot, bf16x8 (&A)[4][2], s16x4 (&B)[4][2][2]) {
    const int q = slot >> 1, o = (slot & 1) * G2_TILE;
    const int s = j / 12, k = j % 12;
    if (k < 4) A[k][s] = lds_b128(rA[q][k >> 1] + o + (k & 1) * 1024 + s * 8192);
    else B[(k - 4) >> 1][s][k & 1] = lds_tr(rB[q][(k - 4) >> 1] + o + (32 * s + 16 * (k & 1)) * 256);
  };
  auto step = [&](int nslot, bf16x8 (&A)[4][2], s16x4 (&B)[4][2][2], bf16x8 (&An)[4][2], s16x4 (&Bn)[4][2][2]) {
    constexpr int NM = 32;
#pragma unroll
    for (int j = 0; j < NM; ++j) {
      const int p = j >> 1, m = p >> 2, n = (m & 1) ? 3 - (p & 3) : p & 3, s = (j ^ p) & 1;
      const bf16x8 bb = __builtin_bit_cast(bf16x8, __builtin_shufflevector(B[n][s][0], B[n][s][1], 0, 1, 2, 3, 4, 5, 6, 7));
      asm volatile("v_mfma_f32_16x16x32_bf16 %0, %1, %2, %0" : "+v"(acc[m][n]) : "v"(bb), "v"(A[m][s]));
      if (j < 24) rd(j, nslot, An, Bn);
      if (j == NM - 2 && nslot == 0) WAIT_V(0);
      if (j == NM - 1) {
        WAIT_L0(); BAR();
      }
      SCHED();
    }
  };
  const int tile = rt * 8 + ctile;
  auto hstep = [&](int nslot, int pub, bf16x8 (&A)[4][2], s16x4 (&B)[4][2][2], bf16x8 (&An)[4][2], s16x4 (&Bn)[4][2][2]) {
#pragma unroll
    for (int j = 0; j < 16; ++j) {
      const int p = j >> 1, m = p >> 2, n = (m & 1) ? 3 - (p & 3) : p & 3, s = (j ^ p) & 1;
      const bf16x8 bb = __builtin_bit_cast(bf16x8, __builtin_shufflevector(B[n][s][0], B[n][s][1], 0, 1, 2, 3, 4, 5, 6, 7));
      asm volatile("v_mfma_f32_16x16x32_bf16 %0, %1, %2, %0" : "+v"(acc[m][n]) : "v"(bb), "v"(A[m][s]));
      if (j < 10) { const int r = (j / 5) * 12 + (j % 5) * 2 + (j % 5 ? 2 : 0); rd(r, nslot, An, Bn); rd(r + 1, nslot, An, Bn); }
      if (j == 12 && pub) { WAIT_V(0); if (lane == 0) __hip_atomic_store((gu32*)g.flags + FL_P2 + tile * 32 + kh * 4 + wq, tag, RLX_AGENT); }
      if (j == 15) { WAIT_L0(); BAR(); }
      SCHED();
    }
  };
  if (type == 0) {
    if (wq == 0) wait_flags8((gu32*)g.flags + FL_RDY + ((rt >> 1) * 4 + kh) * 32, tag, lane);
    BAR();
  }
  BAR();
#pragma unroll
  for (int j = 0; j < 24; ++j) rd(j, 0, A0, B0);
  WAIT_L0(); BAR(); SCHED();
#pragma unroll 1
  for (int t = 4; t <= nt; t += 4) { step(1, A0, B0, A1, B1); step(2, A1, B1, A0, B0); step(3, A0, B0, A1, B1); step(0, A1, B1, A0, B0); }
  constexpr int S0 = (G2_D + G2_E) & 2;
  if (S0) { step(1, A0, B0, A1, B1); step(2, A1, B1, A0, B0); }
  asm volatile("s_nop 7" ::: "memory");
  SCHED();
  {
    const auto rp = __builtin_amdgcn_make_buffer_rsrc((void*)(g.p2 + (size_t)tile * 32768), 0, 32768, 0x00020000);
#pragma unroll
    for (int j = 0; j < 2; ++j)
#pragma unroll
      for (int n = 0; n < 4; n += 2) store16_dev(__builtin_bit_cast(u32x4, pack8(acc[2 + j][n], acc[2 + j][n + 1])), rp, lane * 16, kh * 16384 + wq * 4096 + j * 2048 + n * 512);
  }
  unsigned sw[4];
#pragma unroll
  for (int n = 0; n < 4; ++n) { sw[n] = wq * 8192 + i16 * 256 + (((n * 4 + g4) ^ i16) << 4); asm volatile("" : "+v"(sw[n])); }
  asm volatile("s_nop 1");
  SCHED();
  hstep((S0 + 1) & 3, 0, A0, B0, A1, B1); hstep((S0 + 2) & 3, 1, A1, B1, A0, B0);
  hstep((S0 + 3) & 3, 0, A0, B0, A1, B1); hstep(S0, 0, A1, B1, A0, B0);
  asm volatile("s_nop 7\n\ts_nop 7" ::: "memory");
  SCHED();
#pragma unroll
  for (int j = 0; j < 2; ++j)
#pragma unroll
    for (int n = 0; n < 4; ++n) *(LAS f32x4*)(size_t)(sw[n] + j * 4096) = acc[j][n];
  WAIT_L0(); BAR();
}

__global__ __launch_bounds__(512, 2) void fused_kernel(Args g) {
  __shared__ __attribute__((aligned(16))) char smem[G1_LDS];
  asm volatile("" ::"v"((unsigned)(size_t)(LAS char*)smem));
  const int b = blockIdx.x, xcd = b & 7, slot = b >> 3, kh = xcd >> 2, rh = (xcd >> 1) & 1, ch = xcd & 1;
  const int panel = 4 * rh + (slot & 3), c = slot >> 2, type = c & 1, ct = 8 * kh + 4 * ch + (c >> 1);
  const int rot = ((panel + (c >> 1) + c) & 1) | (rh << 3);
  {
    const int lane = threadIdx.x & 63, w = __builtin_amdgcn_readfirstlane(threadIdx.x >> 6), grp = w >> 2, wq = w & 3;
    if (w == 7) {
      unsigned long long pc; asm volatile("s_getpc_b64 %0" : "=s"(pc));
      pf_line(G1_X + 11264, lane < CODE_LINES ? lane * 128 : OOB, make_srd((const void*)pc, 32 * CODE_LINES * 128), slot * (CODE_LINES * 128));
    }
    const unsigned v0 = (lane >> 3) * (XL * 2) + (((lane & 7) ^ ((lane >> 4) & 7)) << 4), v1 = (8 + (lane >> 3)) * (XL * 2) - 1024 + (((lane & 7) ^ ((4 + (lane >> 4)) & 7)) << 4);
#pragma unroll
    for (int h = 0; h < 2; ++h)
      asm volatile("s_mov_b32 m0, %0\n\ts_nop 0\n\tbuffer_load_dwordx4 %1, %3, %4 offen lds\n\tbuffer_load_dwordx4 %2, %3, %4 offen offset:1024 lds"
                   ::"s"(G1_A + (1 - grp) * 32768 + wq * 4096 + 2 * h * 1024), "v"(v0), "v"(v1), "s"(make_srd(g.x, S_ * XL * 2)),
                     "s"((unsigned)(panel * 256 + (1 - grp) * 128 + 32 * wq + 2 * h * 8) * (XL * 2u) + rot * 128) : "memory");
  }
  const G2Tile T{kh, 2 * panel + (type ^ kh), 4 * ch + (c >> 1), type ? 36 - G2_E - G2_D : 36 - G2_E + G2_D, ((c >> 1) + panel) & 1};
  const unsigned tag = __builtin_expect(type == 0, 1) ? gemm1_body<0>(g, panel, ct, rot, T) : gemm1_body<1>(g, panel, ct, rot, T);
  gemm2_body(g, T, type, (gu32*)g.flags + FL_RDY + (panel * 4 + type * 2 + kh) * 32 + (ct & 7), tag);
}

extern "C" void kernel_launch(void* const* d_in, const int* in_sizes, int n_in, void* d_out, int out_size, void* d_ws, size_t ws_size, hipStream_t stream) {
  const u16 *x = (const u16*)d_in[0], *wqkv = (const u16*)d_in[1], *wphi = (const u16*)d_in[2], *wo = (const u16*)d_in[3];
  const u16 *w1 = (const u16*)d_in[5], *b1 = (const u16*)d_in[6], *w2 = (const u16*)d_in[7], *b2 = (const u16*)d_in[8];
  char* ws = (char*)d_ws;
  Args a{w2, wo, wqkv, wphi, w1, b1, (u16*)(ws + WS_OH), ws + WS_KVP, (unsigned*)(ws + WS_FLAG), x, b2, (u16*)d_out, ws + WS_P2};
  fused_kernel<<<256, 512, 0, stream>>>(a);
}
```
